```python
import torch

def _softmax(x, axis=-1):
    return torch.softmax(x, dim=axis)

def _gelu(x):
    return torch.nn.functional.gelu(x, approximate='tanh')

def _silu(x):
    return torch.nn.functional.silu(x)

def _sigmoid(x):
    return torch.sigmoid(x)

def _layernorm(x, axis=-1, eps=1e-05):
    m = x.mean(dim=axis, keepdim=True)
    v = x.var(dim=axis, keepdim=True, unbiased=False)
    return (x - m) / torch.sqrt(v + eps)

def _scatter_add(tgt, idx, src, axis=0):
    return tgt.clone().index_add_(axis, idx, src)

def _segment_sum(src, seg, n):
    out = torch.zeros((n,) + src.shape[1:], dtype=src.dtype)
    return out.index_add_(0, seg, src)

def _pad(x, pw):
    flat = tuple((v for pair in reversed(pw) for v in pair))
    return torch.nn.functional.pad(x, flat)
kernel_name = 'adaLN_cond__sliding_block_cached__dense_activation_relu__prompt'

def setup_inputs():

    _w = lambda a: (lambda t: t.to(torch.bfloat16) if t.is_floating_point() else t)(torch.as_tensor(a))

    def _A_setup():
        B, Sq, D = (1, 2048, 448)
        Hq, Hkv, Sk, Dh = (16, 8, 2048, 256)
        assert Hq % Hkv == 0, (Hq, Hkv)
        return {'x': _w((lambda *s: torch.randn(*s, dtype=torch.bfloat16) * 0.05)(B, Sq, D)), 'k_cache': _w((lambda *s: torch.randn(*s, dtype=torch.bfloat16) * 3.2)(B, Hkv, Sk, Dh)), 'v_cache': _w((lambda *s: torch.randn(*s, dtype=torch.bfloat16) * 0.4)(B, Hkv, Sk, Dh)), 'wq': _w((lambda *s: torch.randn(*s, dtype=torch.bfloat16) * 3.2)(D, Hq * Dh) * D ** (-0.5)), 'wo': _w((lambda *s: torch.randn(*s, dtype=torch.bfloat16) * 0.4)(Hq * Dh, D) * (Hq * Dh) ** (-0.5))}
    _A_in = _A_setup()

    def _B_setup():
        B, S, D, Dff = (1, 2048, 448, 1792)
        return {'x': _w((lambda *s: torch.randn(*s, dtype=torch.bfloat16) * 0.05)(B, S, D)), 'w1': _w((lambda *s: torch.randn(*s, dtype=torch.bfloat16) * 0.05)(D, Dff)), 'w2': _w((lambda *s: torch.randn(*s, dtype=torch.bfloat16) * 0.05)(Dff, D))}
    _B_in = _B_setup()
    return {'A_x': _A_in['x'], 'A_k_cache': _A_in['k_cache'], 'A_v_cache': _A_in['v_cache'], 'A_wq': _A_in['wq'], 'A_wo': _A_in['wo'], 'B_x': _B_in['x'], 'B_w1': _B_in['w1'], 'B_w2': _B_in['w2'], 'c': _w((lambda *s: torch.randn(*s, dtype=torch.bfloat16) * 0.1)(1, 448) * 8.0), 'w_mod': _w((lambda *s: torch.randn(*s, dtype=torch.bfloat16) * 0.1)(448, 6 * 448) * 448 ** (-0.5) * 8.0)}

def reference(A_x, A_k_cache, A_v_cache, A_wq, A_wo, B_x, B_w1, B_w2, c, w_mod):

    def _A_ref(x, k_cache, v_cache, wq, wo):
        B, Sq, D = x.shape
        Hq, Dh = (16, 256)
        Hkv, Sk = (k_cache.shape[1], k_cache.shape[2])
        W = min(4096, max(256, Sk // 4))
        rep = Hq // Hkv
        q = (lambda x, *d: x.permute(*d))((x @ wq).reshape(B, Sq, Hq, Dh), 0, 2, 1, 3)
        k = (lambda x, n, axis: torch.repeat_interleave(x, n, dim=axis))(k_cache[:, :, Sk - W:, :], rep, axis=1)
        v = (lambda x, n, axis: torch.repeat_interleave(x, n, dim=axis))(v_cache[:, :, Sk - W:, :], rep, axis=1)
        scores = torch.einsum('bhqd,bhkd->bhqk', q, k) * Dh ** (-0.5)
        p = _softmax(scores, axis=-1)
        o = torch.einsum('bhqk,bhkd->bhqd', p, v)
        return (lambda x, *d: x.permute(*d))(o, 0, 2, 1, 3).reshape(B, Sq, Hq * Dh) @ wo

    def _B_ref(x, w1, w2):
        h = torch.einsum('bsd,df->bsf', x, w1)
        zero = torch.zeros_like(h)
        h = torch.maximum(h, zero)
        return torch.einsum('bsf,fd->bsd', h, w2)
    x = A_x
    mod = (_silu(c) @ w_mod).reshape(c.shape[0], 6, 1, -1)
    sa, ca, ga, sb, cb, gb = (mod[:, 0].clone(), mod[:, 1].clone(), mod[:, 2].clone(), mod[:, 3].clone(), mod[:, 4].clone(), mod[:, 5].clone())
    h = _layernorm(x, axis=-1) * (1 + ca) + sa
    x = x + ga * _A_ref(h, A_k_cache, A_v_cache, A_wq, A_wo)
    h = _layernorm(x, axis=-1) * (1 + cb) + sb
    x = x + gb * _B_ref(h, B_w1, B_w2)
    return x
```

```cpp
#include <hip/hip_runtime.h>
#include <hip/hip_bf16.h>
#include <utility>

typedef __attribute__((ext_vector_type(4))) short s4;
typedef __attribute__((ext_vector_type(4))) int i4;
typedef __attribute__((ext_vector_type(4))) unsigned u4;
typedef __attribute__((ext_vector_type(4))) float f4;
typedef unsigned short bf16_t;
#define LDSP(T) __attribute__((address_space(3))) T*
#define DEV __device__ __forceinline__

template <int V> using ic = std::integral_constant<int, V>;
template <class F, int... I> DEV void sfor_(F&& f, std::integer_sequence<int, I...>) { (f(ic<I>{}), ...); }
template <int N, class F> DEV void sfor(F&& f) { sfor_(f, std::make_integer_sequence<int, N>{}); }

constexpr int FM_TILE = 56 * 512;
constexpr int FM_BYTES = (1 << 21) + 4224;
constexpr size_t WS_MOD = 0;
constexpr size_t WS_FLAG = 16384;
constexpr size_t WS_N = 65536;
constexpr size_t WS_H2 = WS_N + FM_BYTES;
constexpr size_t WS_PA = WS_H2 + FM_BYTES;
constexpr size_t WS_PF = WS_PA + 8 * (size_t)FM_BYTES;
constexpr int R4 = 131072;
constexpr int LDS_BYTES = 163840;
constexpr int PD = 4;

DEV float bflo(unsigned v) { return __uint_as_float(v << 16); }
DEV float bfhi(unsigned v) { return __uint_as_float(v & 0xffff0000u); }
DEV unsigned pk2(float a, float b) { unsigned r; asm("v_cvt_pk_bf16_f32 %0, %1, %2" : "=v"(r) : "v"(a), "v"(b)); return r; }
DEV float rbf(float a) { return bflo(pk2(a, a)); }
DEV i4 mksrd(const void* p, unsigned bytes) {
  i4 s; s[0] = __builtin_amdgcn_readfirstlane((int)(size_t)p); s[1] = __builtin_amdgcn_readfirstlane((int)((size_t)p >> 32));
  s[2] = __builtin_amdgcn_readfirstlane((int)bytes); s[3] = 0x20000; return s;
}
DEV float vfma(float a, float b, float c) { float r; asm volatile("v_fma_f32 %0, %1, %2, %3" : "=v"(r) : "v"(a), "v"(b), "v"(c)); return r; }
DEV float vmul(float a, float b) { float r; asm volatile("v_mul_f32 %0, %1, %2" : "=v"(r) : "v"(a), "v"(b)); return r; }
DEV float vexp(float a) { float r; asm volatile("v_exp_f32 %0, %1" : "=v"(r) : "v"(a)); return r; }
DEV void vmax3(float& m, float a, float b) { asm volatile("v_max3_f32 %0, %0, %1, %2" : "+v"(m) : "v"(a), "v"(b)); }
DEV unsigned vcvt(float a, float b) { unsigned r; asm volatile("v_cvt_pk_bf16_f32 %0, %1, %2" : "=v"(r) : "v"(a), "v"(b)); return r; }
DEV float vlo(unsigned a) { float r; asm volatile("v_lshlrev_b32 %0, 16, %1" : "=v"(r) : "v"(a)); return r; }
DEV float vhi(unsigned a) { float r; asm volatile("v_and_b32 %0, 0xffff0000, %1" : "=v"(r) : "v"(a)); return r; }
DEV float ard(float a) { float r; asm volatile("v_accvgpr_read_b32 %0, %1" : "=v"(r) : "a"(a)); return r; }
DEV unsigned awr(unsigned a) { unsigned r; asm volatile("v_accvgpr_write_b32 %0, %1" : "=a"(r) : "v"(a)); return r; }
DEV void vmod_a(float& x, float& y, unsigned n, float c0, float s0) {
  asm volatile("v_lshlrev_b32 %0, 16, %2\n\tv_and_b32 %1, 0xffff0000, %2\n\tv_fma_f32 %0, %0, %3, %4" : "=&v"(x), "=&v"(y) : "v"(n), "v"(c0), "v"(s0));
}
DEV unsigned vmod_b(float x, float y, float c1, float s1) {
  unsigned r;
  asm volatile("v_fma_f32 %0, %2, %3, %4\n\tv_cvt_pk_bf16_f32 %0, %1, %0" : "=&v"(r) : "v"(x), "v"(y), "v"(c1), "v"(s1));
  return r;
}
DEV unsigned acvt(float a, float b) {
  unsigned r, t;
  asm volatile("v_accvgpr_read_b32 %0, %2\n\tv_accvgpr_read_b32 %1, %3\n\tv_cvt_pk_bf16_f32 %0, %0, %1" : "=&v"(r), "=&v"(t) : "a"(a), "a"(b));
  return r;
}
DEV float vmulfma(float a, float x, float b, float y) {
  float r;
  asm volatile("v_mul_f32 %0, %1, %2\n\tv_fma_f32 %0, %3, %4, %0" : "=&v"(r) : "v"(a), "v"(x), "v"(b), "v"(y));
  return r;
}
DEV void vmax8(float& m, f4 a, f4 b) {
  asm volatile("v_max3_f32 %0, %0, %1, %2\n\tv_max3_f32 %0, %0, %3, %4\n\tv_max3_f32 %0, %0, %5, %6\n\tv_max3_f32 %0, %0, %7, %8"
               : "+v"(m) : "v"(a[0]), "v"(a[1]), "v"(a[2]), "v"(a[3]), "v"(b[0]), "v"(b[1]), "v"(b[2]), "v"(b[3]));
}
DEV void touch(u4 a) { asm volatile("" ::"v"(a)); }
DEV void cvt4(unsigned* d, f4 a, f4 b) {
  asm volatile("v_cvt_pk_bf16_f32 %0, %4, %5\n\tv_cvt_pk_bf16_f32 %1, %6, %7\n\tv_cvt_pk_bf16_f32 %2, %8, %9\n\tv_cvt_pk_bf16_f32 %3, %10, %11"
               : "=&v"(d[0]), "=&v"(d[1]), "=&v"(d[2]), "=&v"(d[3]) : "v"(a[0]), "v"(a[1]), "v"(a[2]), "v"(a[3]), "v"(b[0]), "v"(b[1]), "v"(b[2]), "v"(b[3]));
}
DEV void swap4_a(unsigned* q, unsigned* d) {
  asm volatile("v_permlane16_swap_b32 %4, %6\n\tv_permlane16_swap_b32 %5, %7\n\tv_accvgpr_write_b32 %0, %4\n\tv_accvgpr_write_b32 %2, %6\n\t"
               "v_accvgpr_write_b32 %1, %5\n\tv_accvgpr_write_b32 %3, %7"
               : "=a"(q[0]), "=a"(q[1]), "=a"(q[2]), "=a"(q[3]), "+v"(d[0]), "+v"(d[1]), "+v"(d[2]), "+v"(d[3]));
}
DEV unsigned vrelucvt(float a, float b) {
  unsigned r, t;
  asm volatile("v_max_f32 %0, 0, %2\n\tv_max_f32 %1, 0, %3\n\tv_cvt_pk_bf16_f32 %0, %0, %1" : "=&v"(r), "=&v"(t) : "v"(a), "v"(b));
  return r;
}
DEV u4 mk4(const unsigned* p) { return u4{p[0], p[1], p[2], p[3]}; }
DEV void opaque(unsigned& a) { asm volatile("" : "+v"(a)); }

template <bool CA, bool BA, bool INIT> DEV void mfma(f4& c, u4 a, u4 b) {
#define MF_(CC, BC) \
  if constexpr (INIT) asm volatile("v_mfma_f32_16x16x32_bf16 %0, %1, %2, 0" : "=&" CC(c) : "v"(a), BC(b)); \
  else asm volatile("v_mfma_f32_16x16x32_bf16 %0, %1, %2, %0" : "+" CC(c) : "v"(a), BC(b));
  if constexpr (CA && BA) { MF_("a", "a") } else if constexpr (CA) { MF_("a", "v") }
  else if constexpr (BA) { MF_("v", "a") } else { MF_("v", "v") }
#undef MF_
}
template <bool INIT, bool BA> DEV void summm(f4& c, u4 a, u4 b) {
  if constexpr (INIT) asm volatile("v_mfma_f32_16x16x32_bf16 %0, %1, %2, 0" : "=&a"(c) : "a"(a), "a"(b));
  else if constexpr (BA) asm volatile("v_mfma_f32_16x16x32_bf16 %0, %1, %2, %0" : "+a"(c) : "a"(a), "a"(b));
  else asm volatile("v_mfma_f32_16x16x32_bf16 %0, %1, %2, %0" : "+a"(c) : "a"(a), "v"(b));
}
typedef __attribute__((ext_vector_type(2))) unsigned u2;
typedef __attribute__((ext_vector_type(2))) float f2;
template <int OFF> DEV u2 rdtr(unsigned base) { return __builtin_bit_cast(u2, __builtin_amdgcn_ds_read_tr16_b64_v4i16((LDSP(s4))(size_t)(base + OFF))); }
template <int OFF> DEV u2 rd64(unsigned base) { return *(LDSP(u2))(size_t)(base + OFF); }
struct Acc { f4 v[2][2]; };
#define EL(X, F, J) (X)[(F) >> 1].v[(J) >> 2][(F) & 1][(J) & 3]
template <int OFF> DEV u4 rd128(unsigned base) { return *(LDSP(u4))(size_t)(base + OFF); }
template <int OFF> DEV void wr128(unsigned base, u4 v) { *(LDSP(u4))(size_t)(base + OFF) = v; }
template <int M0OFF> DEV void setm0(unsigned wbase) { asm volatile("s_add_u32 m0, %0, %1" ::"s"(wbase), "i"(M0OFF) : "memory", "scc"); }
template <int IMM> DEV void dma(unsigned voff, i4 srd, int soff) {
  asm volatile("buffer_load_dwordx4 %0, %1, %2 offen offset:%3 lds" ::"v"(voff), "s"(srd), "s"(soff), "i"(IMM) : "memory");
}
template <int SOFF> DEV void gstore(u4 d, unsigned voff, i4 srd) {
  unsigned so;
  asm volatile("s_mov_b32 %0, %4\n\tbuffer_store_dwordx4 %1, %2, %3, %0 offen sc1\n\ts_nop 1" : "=&s"(so) : "v"(d), "v"(voff), "s"(srd), "i"(SOFF) : "memory");
}
template <int VM> DEV void bar() { asm volatile("s_waitcnt vmcnt(%0) lgkmcnt(0)\n\ts_barrier" ::"i"(VM) : "memory"); }
DEV void xbar() { asm volatile("s_waitcnt lgkmcnt(0)\n\ts_barrier" ::: "memory"); }
DEV void landed1(u4& a) { asm volatile("" : "+v"(a)); }
DEV void landed(u4& a, u4& b, u4& c, u4& d) { asm volatile("" : "+v"(a), "+v"(b), "+v"(c), "+v"(d)); }
DEV float xhalf_max(float m) {
  auto r = __builtin_amdgcn_permlane32_swap(__float_as_uint(m), __float_as_uint(m), false, false);
  return fmaxf(__uint_as_float(r[0]), __uint_as_float(r[1]));
}
DEV float xhalf_sum(float m) {
  auto r = __builtin_amdgcn_permlane32_swap(__float_as_uint(m), __float_as_uint(m), false, false);
  return __uint_as_float(r[0]) + __uint_as_float(r[1]);
}
DEV float xq_max(float m) {
  auto r = __builtin_amdgcn_permlane16_swap(__float_as_uint(m), __float_as_uint(m), false, false);
  return xhalf_max(fmaxf(__uint_as_float(r[0]), __uint_as_float(r[1])));
}
DEV float xq_sum(float m) {
  auto r = __builtin_amdgcn_permlane16_swap(__float_as_uint(m), __float_as_uint(m), false, false);
  return xhalf_sum(__uint_as_float(r[0]) + __uint_as_float(r[1]));
}
DEV unsigned tr_base(int l, int t4) { return (l >> 2) * 128 + ((t4 ^ ((l >> 3) & 3)) * 32) + (l & 3) * 8; }
DEV unsigned tr_voff(int l, int w, int stride) { return ((l >> 3) + 8 * w) * stride + (((l & 7) ^ ((l >> 4) << 1)) * 16); }

DEV void unpack8(u4 v, float* x) {
#pragma unroll
  for (int k = 0; k < 4; k++) { x[2 * k] = bflo(v[k]); x[2 * k + 1] = bfhi(v[k]); }
}
DEV u4 pack8(const float* x) { u4 r; r[0] = pk2(x[0], x[1]); r[1] = pk2(x[2], x[3]); r[2] = pk2(x[4], x[5]); r[3] = pk2(x[6], x[7]); return r; }
DEV int ucol(int u) { return 32 * (u >> 2) + 4 * (u & 3); }
DEV void stu(bf16_t* p, u4 v) { *(u2*)p = u2{v[0], v[1]}; *(u2*)(p + 16) = u2{v[2], v[3]}; }
template <int OFF> DEV void ld8f(unsigned p, float* x) { const f4 a = __builtin_bit_cast(f4, rd128<OFF>(p)), b = __builtin_bit_cast(f4, rd128<OFF + 64>(p));
#pragma unroll
  for (int k = 0; k < 4; k++) { x[k] = a[k]; x[4 + k] = b[k]; } }

template <int SOFF> DEV void gload(u4& d, unsigned voff, i4 srd) {
  asm volatile("buffer_load_dwordx4 %0, %1, %2, %3 offen sc1" : "=v"(d) : "v"(voff), "s"(srd), "s"(SOFF) : "memory");
}
DEV float grp_sum(float v) {
  v += __uint_as_float(__builtin_amdgcn_update_dpp(0u, __float_as_uint(v), 0x128, 0xf, 0xf, false));
  return xq_sum(v);
}
template <int YOUNGER = 0> DEV void publish(unsigned* fl, int g) {
  bar<YOUNGER>();
  if (threadIdx.x == 0) __hip_atomic_store(fl + g, 1u, __ATOMIC_RELAXED, __HIP_MEMORY_SCOPE_AGENT);
}
DEV void await8(unsigned* fl, int w, int l, int g) {
  if (w == 0) {
    for (int it = 0; it < (1 << 18); ++it) {
      const unsigned v = __hip_atomic_load(fl + (l & 7), __ATOMIC_RELAXED, __HIP_MEMORY_SCOPE_AGENT);
      if (__builtin_amdgcn_ballot_w64(v != 1u && (l & 7) != g) == 0) break;
      __builtin_amdgcn_s_sleep(1);
    }
  }
  asm volatile("s_barrier" ::: "memory");
}
template <int NP> DEV void sum_parts(char* base, unsigned oa, unsigned ob, float* a) {
  const __amdgpu_buffer_rsrc_t r = __builtin_amdgcn_make_buffer_rsrc(base, 0, NP * FM_BYTES, 0x20000);
  u4 pv[NP][2];
#pragma unroll
  for (int i = 0; i < NP; i++) {
    pv[i][0] = __builtin_amdgcn_raw_buffer_load_b128(r, oa, i * FM_BYTES, 16);
    pv[i][1] = __builtin_amdgcn_raw_buffer_load_b128(r, ob, i * FM_BYTES, 16);
  }
#pragma unroll
  for (int k = 0; k < 16; k++) a[k] = 0.f;
#pragma unroll
  for (int i = 0; i < NP; i++) { float x16[16]; unpack8(pv[i][0], x16); unpack8(pv[i][1], x16 + 8);
#pragma unroll
    for (int k = 0; k < 16; k++) a[k] += x16[k]; }
}
constexpr int F_wop(int wn, int rel) {
  if (wn == 0) return 2008 + rel - 4;
  if (wn <= 10 && (rel & 1)) return (44 + wn + 3) * 16 + rel / 2;
  if (wn == 1) return 2020 + rel / 2;
  if (wn == 13 && rel == 1) return 995;
  if (wn == 13 && rel == 15) return 996;
  if (wn >= 8 && (rel == 10 || rel == 14)) return 1100 + 2 * (wn - 8) + (rel - 10) / 4;
  return -1;
}
constexpr int F_op(int t) { return t + PD >= 224 ? -1 : F_wop((t + PD) >> 4, (t + PD) & 15); }
constexpr bool F_need(int n, int c) { return c >= 2000 ? (c - 2000) / 4 <= n : (c < 1000 && c / 16 == 44 + n); }
constexpr int F_wait(int n) {
  int cnt = 0;
  for (int i = 0; i < 8; i++) cnt = F_need(n, 2000 + i) ? 0 : cnt + 1;
  for (int t = 0; t < 16 * n - PD; t++) { int c = F_op(t); if (c >= 0) cnt = F_need(n, c) ? 0 : cnt + 1; }
  return cnt;
}

DEV void st1(unsigned v, unsigned voff, i4 srd, int soff) { asm volatile("buffer_store_dword %0, %1, %2, %3 offen sc1" ::"v"(v), "v"(voff), "s"(srd), "s"(soff) : "memory"); }
DEV void ld1(unsigned& v, unsigned voff, i4 srd, int soff) { asm volatile("buffer_load_dword %0, %1, %2, %3 offen sc1" : "=v"(v) : "v"(voff), "s"(srd), "s"(soff) : "memory"); }
DEV void pf(unsigned& junk, unsigned voff, i4 srd) { asm volatile("buffer_load_dword %0, %1, %2, 0 offen" : "+v"(junk) : "v"(voff), "s"(srd) : "memory"); }

constexpr int A_start(int n) { return n < 14 ? 16 * n : n <= 28 ? 224 + 32 * (n - 14) : n == 29 ? 736 : 736 + 16 * (n - 30); }
constexpr int A_stage(int t) { return t < 224 ? t / 16 : t < 672 ? 14 + (t - 224) / 32 : t < 736 ? 28 : 30 + (t - 736) / 16; }
constexpr int A_wop(int wn, int rel) {
  if (wn <= 13 || wn >= 31) { if (rel & 1) return (wn + 3) * 16 + rel / 2; if (wn <= 9 && rel == 8) return (wn + 4) * 16 + 8; }
  else if (wn <= 28) { if (rel % 4 == 1 && rel < 32) return (wn + 3) * 16 + rel / 4; if ((wn == 14 || wn == 18) && rel == 31) return 990 + (wn - 14) / 4; }
  else if (wn == 30) return (32 + rel / 8) * 16 + rel % 8;
  if (wn >= 35 && wn <= 38 && rel == 6) return 981 + wn - 35;
  if (wn == 42 && rel == 2) return 993;
  if (wn == 43 && rel == 2) return 994;
  if (wn >= 33 && (wn & 1) && (rel == 4 || rel == 12)) return 1000 + (wn - 33) + rel / 8;
  return -1;
}
constexpr int A_op(int t) { return t + PD >= 960 ? -1 : A_wop(A_stage(t + PD), t + PD - A_start(A_stage(t + PD))); }
constexpr int A_NPRO = 31, A_PRO_B0 = 23;
constexpr int A_pro(int i) {
  return i == 0 ? 980 : i == 1 ? 8 : i == 2 ? 24 : i < 11 ? i - 3 : i == 11 ? 40 : i < 20 ? 16 + i - 12 : i == 20 ? 56 : i < 29 ? 32 + i - 21 : 48 + i - 29;
}
constexpr bool A_need(int n, int c) { return c == 980 || (c < 980 && (((c & 15) < 8 && (c / 16 == n || (n == 28 && c / 16 == 29))) || ((c & 15) == 8 && c / 16 <= n + 1))); }
constexpr int A_wait(int n) {
  int cnt = 0;
  for (int i = 0; i < (n == 0 ? A_PRO_B0 : A_NPRO); i++) cnt = A_need(n, A_pro(i)) ? 0 : cnt + 1;
  for (int t = 0; t < A_start(n) - PD; t++) { int c = A_op(t); if (c >= 0) cnt = A_need(n, c) ? 0 : cnt + 1; }
  return cnt;
}
constexpr int A_after(int c) { int k = 0; for (int t = 900; t < 960; t++) if (A_op(t) >= 0) k = A_op(t) == c ? 0 : k + 1; return k; }
constexpr int SM_N0 = 112;
constexpr int SM_e(int t, int lag) {
  if (t < 360 || t == 480) return -1;
  if (t < 480) { int e = t - 360 - lag; return e >= 0 && e < SM_N0 ? e : -1; }
  if (t % 4 == 1 && t % 20 != 1) return -1;
  int e = SM_N0 - lag;
  for (int u = 481; u < t; u++) if (!(u % 4 == 1 && u % 20 != 1)) e++;
  return e >= SM_N0 && e < 256 ? e : -1;
}

extern "C" __global__ __launch_bounds__(256, 1) void k1_attn(const bf16_t* __restrict__ wq, const bf16_t* __restrict__ kc,
                                                  const bf16_t* __restrict__ vc, const bf16_t* __restrict__ wo,
                                                  const bf16_t* __restrict__ w1, const bf16_t* __restrict__ w2,
                                                  const bf16_t* __restrict__ x, bf16_t* __restrict__ out, char* ws) {
  const int l = threadIdx.x & 63, w = __builtin_amdgcn_readfirstlane(threadIdx.x >> 6);
  const int hh = w >> 1, qh = w & 1, g4 = l >> 4;
  const int g = blockIdx.x & 7, rt = blockIdx.x >> 3;
  const i4 sN = mksrd(ws + WS_N + 2 * rt * FM_TILE, 2 * FM_TILE);
  const i4 sM = mksrd(ws + WS_MOD, 2688 * 4);
  const i4 sX = mksrd(x + (64 * rt + 8 * g) * 448, 8 * 896);
  const i4 sQ = mksrd(wq + g * 512, 448 * 8192 - g * 1024);
  const i4 sK = mksrd(kc + (size_t)(g * 2048 + 1536) * 256, 262144);
  const i4 sV = mksrd(vc + (size_t)(g * 2048 + 1536) * 256, 262144);
  const i4 sO = mksrd(wo + (size_t)g * 512 * 448, 512 * 896);
  const i4 sP = mksrd(ws + WS_PA + (size_t)g * FM_BYTES + (2 * rt + qh) * FM_TILE, FM_TILE);
  const bool ffn = g != (rt & 7);
  const int sl = g < 7 ? g : (rt & 7) % 7;
  const i4 s1 = mksrd(w1 + sl * 256, 448 * 3584 - sl * 512);
  const i4 s2 = mksrd(w2 + (size_t)sl * 256 * 448, 256 * 896);
  const i4 sH = mksrd(ws + WS_H2 + (2 * rt + qh) * FM_TILE, FM_TILE);
  const i4 sG = mksrd(ws + WS_PF + (size_t)sl * FM_BYTES + (2 * rt + qh) * FM_TILE, FM_TILE);
  const i4 sF0 = w == 3 ? sV : sO;
  const i4 sF1 = hh ? s2 : s1;
  const i4 sFl = mksrd(ws + WS_FLAG + rt * 128, 4 * 4096 + 128);
  unsigned one, vfs, vfl, fpv;
  const unsigned wb = w * 1024;
  unsigned vq = tr_voff(l, w, 8192), vk, vv, vo[8], v1, vl;
  u4 h2[28], nf[14];
  unsigned vs[2];
  unsigned vm = l * 16 + wb, vn = l * 16 + qh * FM_TILE + hh * 14336;
  unsigned vf0 = (w == 3 ? rt * 64 + l : rt * 112 + w * 38 + (l < 38 ? l : 0)) * 128, junk = 0;
  const int fl = rt * 56 + qh * 28 + (l & 31);
  unsigned vf1 = hh ? fl * 128 : (fl >> 2) * 3584 + (fl & 3) * 128;
  opaque(vq); opaque(vm); opaque(vn); opaque(vf0); opaque(vf1);
  asm volatile("s_nop 4");

  u4 ob[2];
  auto op_m0 = [&](auto C) {
    constexpr int c = C, m = c / 16, j = c & 15, slot = (m & 3) * 32768;
    if constexpr (c == 980) setm0<R4>(wb);
    else if constexpr (c > 980 && c < 985) setm0<R4 + 1024 + (c - 981) * 4096>(wb);
    else if constexpr (c >= 990 || c < 0) {}
    else if constexpr (j == 8) {}
    else if constexpr (m < 14) setm0<slot + j * 4096 - j * 128>(wb);
    else if constexpr (m < 30 || (m >= 44 && m < 51)) setm0<slot + (j & 3) * (8192 - 128) + (j >> 2) * 4096>(wb);
    else setm0<slot + (j & 3) * 8192 + (j >> 2) * 4096>(wb);
  };
  auto op_go = [&](auto C) {
    constexpr int c = C, m = c / 16, j = c & 15;
    if constexpr (c < 0) {}
    else if constexpr (c == 980) dma<0>(vm, sM, 0);
    else if constexpr (c == 990) pf(junk, vf0, sF0);
    else if constexpr (c == 991) pf(junk, vf1, sF1);
    else if constexpr (c == 981 || c == 982) dma<0>(vm, sM, 3584 + (c - 981) * 4096);
    else if constexpr (c == 983 || c == 984) dma<0>(vm, sX, (c - 983) * 4096);
    else if constexpr (c == 993 || c == 995) st1(one, vfs, sFl, (3 + (c - 993) / 2) * 4096);
    else if constexpr (c == 994 || c == 996) ld1(fpv, vfl, sFl, (3 + (c - 994) / 2) * 4096);
    else if constexpr (c >= 2000) gload<(c & 1) * 14336 + ((c - 2000) >> 1) * 1024>(h2[c - 2000], vl, sH);
    else if constexpr (c >= 1100) gstore<((c - 1100) >> 1) * 2048 + (c & 1) * 14336>(ob[c & 1], vm, sG);
    else if constexpr (c >= 1000) gstore<((c - 1000) >> 1) * 2048>(ob[c & 1], vs[c & 1], sP);
    else if constexpr (j == 8) gload<m * 1024>(nf[m], vn, sN);
    else if constexpr (m < 14) dma<j * 128>(vq, sQ, m * 32 * 8192);
    else if constexpr (m < 22) dma<(j & 3) * 128>(vk, sK, (64 * (m - 14) + 32 * (j >> 2)) * 512);
    else if constexpr (m < 30) dma<(j & 3) * 128>(vv, sV, (64 * (m - 22) + 32 * (j >> 2)) * 512);
    else if constexpr (m < 44) dma<0>(vo[j], sO, 128 * (m & 1) * 896 + ((m - 30) >> 1) * 128);
    else if constexpr (m < 51) dma<(j & 3) * 128>(v1, s1, (64 * (m - 44) + 32 * (j >> 2)) * 3584);
    else dma<0>(vo[j], s2, (m - 51) * 128);
  };
  auto op_now = [&](auto C) { op_m0(C); asm volatile("s_nop 0"); op_go(C); };
  sfor<A_PRO_B0>([&](auto I) { op_now(ic<A_pro(I)>{}); });

  unsigned bq[4][2], bk[2][2], bv[4][2], bo[4][2], bm = R4 + g4 * 16, bnw = R4 + 4096 + wb + l * 16, bnr = R4 + 4096 + (wb ^ 2048) + l * 16, bxw, bxr;
  sfor<4>([&](auto T) { sfor<2>([&](auto W) { bq[T][W] = tr_base(l, T) + hh * 16384 + W * 65536; opaque(bq[T][W]); }); });
  opaque(bm); opaque(bnw); opaque(bnr);

  auto aread = [&](auto T_, auto H_) -> u2 {
    constexpr int t = T_, H = H_, n = A_stage(t), i = t - A_start(n), sb = (n & 1) * 32768, W = (n & 3) >> 1;
    if constexpr (n < 14) return rdtr<sb + (i >> 2) * 4096 + H * 2048>(bq[i & 3][W]);
    else if constexpr (n < 28) return rdtr<sb + ((i & 15) >> 2) * 8192 + (i >> 4) * 4096 + H * 2048>(bv[i & 3][W]);
    else if constexpr (n == 28) return rdtr<((i >> 1) & 1) * 32768 + (i >> 4) * 8192 + (i & 1) * 4096 + H * 2048>(bv[(i >> 2) & 3][0]);
    else return rdtr<sb + ((i & 7) >> 1) * 4096 + H * 2048>(bo[2 * (i >> 3) + (i & 1)][W]);
  };

  u4 hfo[14], hfp[14];
  f4 cs[2][4];
  float mt[2];
  auto csread = [&](auto M, auto K) {
    constexpr int m = M, k = K;
    cs[m & 1][k] = __builtin_bit_cast(f4, rd128<m * 128 + (k >> 1) * 64 + ((k & 1) ? 0 : 1792)>(bm));
  };
  auto mod_a = [&](auto M, auto K) { constexpr int m = M, k = K;
    vmod_a(mt[0], mt[1], nf[m][k], cs[m & 1][k & 2][2 * (k & 1)], cs[m & 1][(k & 2) + 1][2 * (k & 1)]); };
  auto mod_b = [&](auto M, auto K) { constexpr int m = M, k = K;
    hfo[m][k] = vmod_b(mt[0], mt[1], cs[m & 1][k & 2][2 * (k & 1) + 1], cs[m & 1][(k & 2) + 1][2 * (k & 1) + 1]); };

  bar<A_wait(0)>();
  asm volatile("" : "+v"(nf[0]), "+v"(nf[1]));
  u4 af[PD + 1];
  u2 alo;
  sfor<8>([&](auto K) { csread(ic<K / 4>{}, ic<K % 4>{}); });
  sfor<PD>([&](auto T) { af[T] = __builtin_shufflevector(aread(T, ic<0>{}), aread(T, ic<1>{}), 0, 1, 2, 3); });
  sfor<4>([&](auto K) { op_now(ic<A_pro(A_PRO_B0 + K)>{}); mod_a(ic<0>{}, K); mod_b(ic<0>{}, K); });
  wr128<0>(bnw, hfo[0]);
  sfor<A_NPRO - A_PRO_B0 - 4>([&](auto P) { op_now(ic<A_pro(A_PRO_B0 + 4 + P)>{}); if constexpr (P == 1) { xbar(); hfp[0] = rd128<0>(bnr); } });

  Acc qa[8], S[16], O[8], oca[2], ocb[2];
  u4 Qf[16], Pf[32], own[16], par[16];
  unsigned Qs[4], qd[4], Ps[32];
  float tf[256], te[256], ot[2];
  float mx0[2] = {-INFINITY, -INFINITY}, mx1[2] = {-INFINITY, -INFINITY}, nm[2], linv[2], linp[2];
  f4 ls[2];
  u4 ones;
  float csc = 0.09016844005556021f;
  asm volatile("" : "+v"(csc), "+v"(mx0[0]), "+v"(mx0[1]), "+v"(mx1[0]), "+v"(mx1[1]));

  sfor<60>([&](auto TO) { sfor<16>([&](auto TI) {
    constexpr int t = TO * 16 + TI, n = A_stage(t), i = t - A_start(n);
    if constexpr (t == 480) {
      asm volatile("s_nop 5");
      vmax8(mx1[0], S[15].v[0][0], S[15].v[1][0]); vmax8(mx1[1], S[15].v[0][1], S[15].v[1][1]);
      const float ov0 = fmaf(mx1[0], csc, nm[0]), ov1 = fmaf(mx1[1], csc, nm[1]);
      if (__builtin_expect(__builtin_amdgcn_ballot_w64(!(ov0 <= 72.f && ov1 <= 72.f)) != 0, 0)) {
        float f[2];
        sfor<2>([&](auto B) {
          const float nm2 = fminf(nm[B], -xq_max(mx1[B]) * csc);
          f[B] = __builtin_amdgcn_exp2f(nm2 - nm[B]); nm[B] = nm2; });
        sfor<SM_N0 / 8>([&](auto F) { sfor<4>([&](auto K) {
          unsigned v;
          if constexpr (F < 8) v = __float_as_uint(ard(__uint_as_float(Pf[F][K + 0]))); else v = Pf[F][K + 0];
          v = vcvt(vmul(vlo(v), f[F & 1]), vmul(vhi(v), f[F & 1]));
          if constexpr (F < 8) Ps[4 * F + K] = awr(v); else Pf[F][K + 0] = v; });
          if constexpr (F < 8) { Pf[F] = mk4(Ps + 4 * F); asm volatile("" : "+a"(Pf[F])); } });
        asm volatile("s_nop 1");
      }
    }
    const u4 a = af[t % (PD + 1)];
    auto mm = [&](auto B) {
      constexpr int nb = B;
      if constexpr (n < 14) mfma<false, false, n == 0>(qa[i >> 1].v[i & 1][nb], a, nb ? hfp[n] : hfo[n]);
      else if constexpr (n < 22) mfma<false, true, (i & 7) == 0>(S[2 * (n - 14) + (i >> 4)].v[(i >> 3) & 1][nb], a, Qf[2 * (i & 7) + nb]);
      else if constexpr (n < 28) { constexpr int G = 2 * (n - 22) + (i >> 4); mfma<true, (G < 4), G == 0>(O[(i & 15) >> 1].v[i & 1][nb], a, Pf[2 * G + nb]); }
      else if constexpr (n == 28) mfma<true, false, false>(O[i >> 3].v[(i >> 2) & 1][nb], a, Pf[24 + 2 * (i & 3) + nb]);
      else {
        constexpr int b = n & 1, jp = (n - 30) >> 1, kb = (i & 7) >> 1;
        if constexpr (i < 8) mfma<false, false, b == 0 && kb == 0>(oca[jp & 1].v[i & 1][nb], a, own[8 * b + 2 * kb + nb]);
        else mfma<false, false, b == 0 && kb == 0>(ocb[jp & 1].v[i & 1][nb], a, par[8 * b + 2 * kb + nb]);
      }
    };
    constexpr int wn = A_stage(t + PD), rel = t + PD - A_start(wn);
    constexpr int e0 = SM_e(t, 0), e1 = SM_e(t, 1), e2 = SM_e(t, 2), e3 = SM_e(t, 3);
    constexpr int oj = (t - 772) / 32, ok = t >= 772 ? (t - 772) % 32 : 99;
    if constexpr ((t & 1) == 0) touch(af[(t + 1) % (PD + 1)]);
    mm(ic<0>{});
    if constexpr (t == 150) { vk = ((l >> 3) + 8 * w) * 512 + (((l & 7) ^ ((g4 & 1) | (qh << 1) | (((g4 >> 1) ^ qh) << 2))) * 16); opaque(vk); }
    if constexpr (t == 206) sfor<2>([&](auto C) { sfor<2>([&](auto W) {
      const int fy = ((l >> 1) & 1) | ((l >> 2) & 2) | (((l >> 2) ^ (l >> 3)) & 1) << 2;
      bk[C][W] = (l & 15) * 128 + (((4 * C + ((g4 & 1) << 1) + (g4 >> 1)) ^ fy) * 16) + W * 65536; opaque(bk[C][W]); }); });
    if constexpr (t == 340) { vv = tr_voff(l, w, 512); opaque(vv); }
    if constexpr (t == 479) asm volatile("" :: "v"(junk));
    if constexpr (t == 478) { sfor<4>([&](auto K) { Qs[K] = awr(0x3f803f80u); }); ones = mk4(Qs); asm volatile("" : "+a"(ones)); }
    if constexpr (t == 475) sfor<4>([&](auto T) { bv[T][1] = tr_base(l, T) + 65536; opaque(bv[T][1]); });
    if constexpr (t == 533) sfor<4>([&](auto T) { bv[T][0] = tr_base(l, T); opaque(bv[T][0]); });
    if constexpr (t == 625) sfor<8>([&](auto J) { vo[J] = tr_voff(l, w, 896) + (256 * ((J & 3) >> 1) + 64 * (J & 1) + 32 * (J >> 2)) * 896; opaque(vo[J]); });
    if constexpr (t == 633) { bxw = R4 + w * 8192 + l * 16; bxr = R4 + (w ^ 2) * 8192 + l * 16; opaque(bxw); opaque(bxr); }
    if constexpr (t == 890) {
      v1 = tr_voff(l, w, 3584); one = 1; vfs = threadIdx.x == 0 ? g * 4 : 0x40000000u; vfl = (l & 7) * 4;
      opaque(v1); opaque(one); opaque(vfs); opaque(vfl);
    }
    if constexpr (t == 673) {
      sfor<4>([&](auto P) { sfor<2>([&](auto W) { bo[P][W] = tr_base(l, 2 * hh + (P & 1)) + (hh ^ (P >> 1)) * 16384 + W * 65536; opaque(bo[P][W]); }); });
      vs[0] = l * 16 + hh * (1024 + 14336); vs[1] = l * 16 + hh * 1024 + (hh ^ 1) * 14336; opaque(vs[0]); opaque(vs[1]);
      vm = l * 16 + wb; opaque(vm);
    }
    if constexpr (t + PD < 960) {
      if constexpr (t == 680 || t == 716 || t == 724) xbar();
      op_m0(ic<A_op(t)>{});
      if constexpr (rel != 0 && (wn < 14 || wn >= 22)) alo = aread(ic<t + PD>{}, ic<0>{});
    }
    if constexpr (wn <= 12 && rel >= 6 && rel <= 12 && (rel & 1) == 0) mod_b(ic<wn + 1>{}, ic<(rel - 6) / 2>{});
    if constexpr (wn <= 12 && rel == 14) wr128<((wn + 1) & 1) * 4096>(bnw, hfo[wn + 1]);
    if constexpr (wn >= 1 && wn <= 12 && rel == 2) { csread(ic<wn + 1>{}, ic<2>{}); csread(ic<wn + 1>{}, ic<3>{}); }
    if constexpr (t >= 212 && t < 228) cvt4(qd, qa[(t - 212) >> 1].v[0][t & 1], qa[(t - 212) >> 1].v[1][t & 1]);
    if constexpr (e1 >= 0) te[e1] = vexp(tf[e1]);
    if constexpr (t >= 244 && t < 480) {
      constexpr int T = (t - 244) / 16, k = (t - 244) % 16, k1 = k - (k + 2) / 4;
      if constexpr (T < 7 && k < 8) vmax3(mx0[k >> 2], EL(S, 2 * T + (k >> 2), 2 * (k & 3)), EL(S, 2 * T + (k >> 2), 2 * (k & 3) + 1));
      if constexpr (T == 7 && k == 0) { vmax8(mx0[0], S[7].v[0][0], S[7].v[1][0]); vmax8(mx0[1], S[7].v[0][1], S[7].v[1][1]); }
      if constexpr (T > 7 && k < 11 && k % 4 != 1) vmax3(mx1[k1 >> 2], EL(S, 2 * T + (k1 >> 2), 2 * (k1 & 3)), EL(S, 2 * T + (k1 >> 2), 2 * (k1 & 3) + 1));
    }
    if constexpr (e3 >= 0 && e3 < 64 && (e3 & 1)) Ps[e3 >> 1] = awr(__float_as_uint(tf[e3]));
    if constexpr (e3 >= 8 && e3 <= 64 && (e3 & 7) == 0) { Pf[e3 / 8 - 1] = mk4(Ps + e3 / 2 - 4); asm volatile("" : "+a"(Pf[e3 / 8 - 1])); }
    if constexpr (t >= 687 && t <= 747 && (t - 687) % 4 == 0) {
      constexpr int f = (t - 687) / 4;
      if constexpr (f != 8 && f != 9) wr128<(f & 7) * 1024>(bxw, own[f]);
    }
    if constexpr (t == 725 || t == 726) wr128<(t - 725) * 1024>(bxw, own[8 + t - 725]);
    if constexpr (t >= 716 && t < 724) par[t - 716] = rd128<((t - 716) ^ 1) * 1024>(bxr);
    if constexpr (t >= 749 && t < 757) par[8 + t - 749] = rd128<((t - 749) ^ 1) * 1024>(bxr);
    if constexpr (ok < 16) ot[ok & 1] = vmulfma(EL(oca + (oj & 1), ok >> 3, ok & 7), linv[ok >> 3], EL(ocb + (oj & 1), ok >> 3, ok & 7), linp[ok >> 3]);
    mm(ic<1>{});
    if constexpr (t + PD < 960) {
      if constexpr (rel == 0) bar<A_wait(wn)>();
      if constexpr (wn >= 14 && wn < 22) af[(t + PD) % (PD + 1)] = rd128<(wn & 1) * 32768 + ((rel & 7) >> 1) * 8192 + (rel >> 3) * 2048>(bk[rel & 1][(wn & 3) >> 1]);
      else {
        if constexpr (rel == 0) alo = aread(ic<t + PD>{}, ic<0>{});
        af[(t + PD) % (PD + 1)] = __builtin_shufflevector(alo, aread(ic<t + PD>{}, ic<1>{}), 0, 1, 2, 3);
      }
      if constexpr (wn >= 1 && wn <= 13 && rel == 0) { hfp[wn] = rd128<(wn & 1) * 4096>(bnr); if constexpr (wn <= 12) landed1(nf[wn + 1]); }
      op_go(ic<A_op(t)>{});
    }
    if constexpr (wn <= 12 && rel >= 4 && rel <= 10 && (rel & 1) == 0) mod_a(ic<wn + 1>{}, ic<(rel - 4) / 2>{});
    if constexpr (wn <= 11 && (rel == 12 || rel == 14)) csread(ic<wn + 2>{}, ic<(rel - 12) / 2>{});
    if constexpr (t >= 212 && t < 228) { swap4_a(Qs, qd); Qf[t - 212] = mk4(Qs); asm volatile("" : "+a"(Qf[t - 212])); }
    if constexpr (t == 357 || t == 358) { nm[t - 357] = -xq_max(mx0[t - 357]) * csc; asm volatile("" : "+v"(nm[t - 357])); }
    if constexpr (e0 >= 0) tf[e0] = vfma(EL(S, e0 >> 3, e0 & 7), csc, nm[(e0 >> 3) & 1]);
    if constexpr (e2 >= 0) {
      if constexpr (e2 & 1) {
        unsigned p = vcvt(te[e2 - 1], te[e2]);
        if constexpr (e2 < 64) tf[e2] = __uint_as_float(p); else Pf[e2 >> 3][(e2 >> 1) & 3] = p;
      }
    }
    static_assert(SM_e(666, 3) < 0 && SM_e(667, 3) < 0 && SM_e(492, 3) < SM_N0 + 15, "exp schedule");
    if constexpr (t >= 485 && (t - 485) % 4 == 0 && t < 485 + 56) summm<(t < 493), (t < 485 + 32)>(ls[((t - 485) / 4) & 1], ones, Pf[(t - 485) / 4]);
    if constexpr (e3 >= SM_N0 && (e3 & 7) == 7) summm<false, false>(ls[(e3 >> 3) & 1], ones, Pf[e3 >> 3]);
    if constexpr (t == 667) { linv[0] = __builtin_amdgcn_rcpf(ard(ls[0][0])); linv[1] = __builtin_amdgcn_rcpf(ard(ls[1][0])); }
    if constexpr (t == 667) *(LDSP(f2))(size_t)bxw = f2{linv[0], linv[1]};
    if constexpr (t == 669) { const f2 r = *(LDSP(f2))(size_t)bxr; linp[0] = r[1]; linp[1] = r[0]; }
    if constexpr (t >= 683 && t < 747) {
      constexpr int k = t - 683;
      own[k >> 2][k & 3] = acvt(EL(O, k >> 2, 2 * (k & 3)), EL(O, k >> 2, 2 * (k & 3) + 1));
    }
    if constexpr (ok < 16 && (ok & 1)) ob[ok >> 3][(ok >> 1) & 3] = vcvt(ot[0], ot[1]);
  }); });
  asm volatile("s_nop 7");
  sfor<2>([&](auto H) {
    float o8[8];
    sfor<8>([&](auto K) { o8[K] = vmulfma(EL(oca, H + 0, K + 0), linv[H], EL(ocb, H + 0, K + 0), linp[H]); });
    sfor<4>([&](auto K) { ob[H][K + 0] = vcvt(o8[2 * K], o8[2 * K + 1]); });
    gstore<6 * 2048>(ob[H], vs[H], sP);
  });

  unsigned* flags = (unsigned*)(ws + WS_FLAG) + rt * 32;
  asm volatile("s_waitcnt vmcnt(%1)" : "+v"(fpv) : "i"(A_after(994) + 2) : "memory");
  const int row = 64 * rt + 8 * g + (l & 7), ua = 14 * w + (l >> 3);
  const bool two = (l >> 3) < 6;
  const int ub = two ? ua + 8 : ua;
  const unsigned fa = (row >> 4) * 14336 + ua * 256 + (row & 15) * 16, fb = two ? fa + 2048 : 0x40000000u;
  const int ca = ucol(ua), cb_ = ucol(ub);
  float x1[16], gb[16];
  unsigned vpf;
  {
    float ga[16], sb[16], cb[16], a[16];
    vpf = (2 * w + (l >> 5)) * FM_BYTES + (4 * rt + (g >> 1)) * 14336 + (l & 31) * 256 + (g & 1) * 128;
    ld1(junk, __builtin_amdgcn_ballot_w64(fpv != 1u) == 0 ? vpf : 0x40000000u, mksrd(ws + WS_PA, 8 * FM_BYTES), 0);
    const unsigned ma = R4 + 1024 + ca * 4, mb = R4 + 1024 + cb_ * 4, xa = R4 + 9216 + (l & 7) * 896;
    unpack8(__builtin_shufflevector(rd64<0>(xa + ca * 2), rd64<32>(xa + ca * 2), 0, 1, 2, 3), x1);
    unpack8(__builtin_shufflevector(rd64<0>(xa + cb_ * 2), rd64<32>(xa + cb_ * 2), 0, 1, 2, 3), x1 + 8);
    ld8f<0>(ma, ga); ld8f<0>(mb, ga + 8); ld8f<1792>(ma, sb); ld8f<1792>(mb, sb + 8);
    ld8f<3584>(ma, cb); ld8f<3584>(mb, cb + 8); ld8f<5376>(ma, gb); ld8f<5376>(mb, gb + 8);
    publish<1>(flags, g);
    await8(flags, w, l, g);
    sum_parts<8>(ws + WS_PA, fa, fb, a);
    float s0 = 0.f, s1_ = 0.f;
#pragma unroll
    for (int k = 0; k < 16; k += 2) {
      const unsigned p = pk2(x1[k] + ga[k] * a[k], x1[k + 1] + ga[k + 1] * a[k + 1]);
      x1[k] = bflo(p); x1[k + 1] = bfhi(p);
      if (k < 8) s0 += x1[k] + x1[k + 1]; else s1_ += x1[k] + x1[k + 1];
    }
    const float mw = grp_sum(two ? s0 + s1_ : s0) * (1.f / 112.f);
    s0 = 0.f; s1_ = 0.f;
#pragma unroll
    for (int k = 0; k < 8; k++) { s0 += (x1[k] - mw) * (x1[k] - mw); s1_ += (x1[k + 8] - mw) * (x1[k + 8] - mw); }
    const float qw = grp_sum(two ? s0 + s1_ : s0);
    if (l < 8) *(LDSP(f2))(size_t)(R4 + l * 32 + w * 8) = f2{mw, qw};
    xbar();
    const f4 r0 = *(LDSP(f4))(size_t)(R4 + (l & 7) * 32), r1 = *(LDSP(f4))(size_t)(R4 + (l & 7) * 32 + 16);
    const float mean = (r0[0] + r0[2] + r1[0] + r1[2]) * 0.25f;
    const float d0 = r0[0] - mean, d1 = r0[2] - mean, d2 = r1[0] - mean, d3 = r1[2] - mean;
    const float var = (r0[1] + r0[3] + r1[1] + r1[3] + 112.f * (d0 * d0 + d1 * d1 + d2 * d2 + d3 * d3)) * (1.f / 448.f);
    const float rs = rsqrtf(var + 1e-5f);
#pragma unroll
    for (int k = 0; k < 16; k++) a[k] = (x1[k] - mean) * rs * cb[k] + sb[k];
    const __amdgpu_buffer_rsrc_t rh = __builtin_amdgcn_make_buffer_rsrc(ws + WS_H2, 0, FM_BYTES, 0x20000);
    __builtin_amdgcn_raw_buffer_store_b128(pack8(a), rh, fa, 0, 16);
    __builtin_amdgcn_raw_buffer_store_b128(pack8(a + 8), rh, fb, 0, 16);
  }
  publish(flags + 1024, g);
  asm volatile("" ::"v"(junk));

  if (ffn) {
    sfor<8>([&](auto J) { op_now(ic<47 * 16 + J>{}); });
    unsigned b1[4][2];
    sfor<4>([&](auto T) { sfor<2>([&](auto W) { b1[T][W] = tr_base(l, T) + hh * 16384 + W * 65536; opaque(b1[T][W]); }); });
    sfor<8>([&](auto J) { vo[J] = tr_voff(l, w, 896) + (64 * (J & 3) + 32 * (J >> 2)) * 896; opaque(vo[J]); });
    vl = l * 16; vm = vl + hh * 1024; opaque(vl); opaque(vm);
    auto fread = [&](auto T_, auto H_) -> u2 {
      constexpr int t = T_, H = H_, n = t >> 4, i = t & 15, sb = (n & 1) * 32768, W = (n & 3) >> 1;
      if constexpr (n < 7) return rdtr<sb + (i >> 3) * 8192 + (i & 1) * 4096 + H * 2048>(b1[(i >> 1) & 3][W]);
      else return rdtr<sb + ((i & 7) >> 1) * 4096 + H * 2048>(bo[2 * (i >> 3) + (i & 1)][W]);
    };
    sfor<PD>([&](auto T) { af[T] = __builtin_shufflevector(fread(T, ic<0>{}), fread(T, ic<1>{}), 0, 1, 2, 3); });
    await8(flags + 1024, w, l, g);
    sfor<8>([&](auto F) { op_go(ic<2000 + F>{}); });
    asm volatile("s_waitcnt vmcnt(%0)" ::"i"(F_wait(0)) : "memory");
    landed(h2[0], h2[1], h2[2], h2[3]);
    Acc ha[4], oc[2];
    sfor<14>([&](auto TO) { sfor<16>([&](auto TI) {
      constexpr int n = TO, i = TI, t = n * 16 + i, wn = (t + PD) >> 4, rel = (t + PD) & 15;
      const u4 a = af[t % (PD + 1)];
      auto mm = [&](auto B) {
        constexpr int nb = B, kb = (i & 7) >> 1;
        if constexpr (n < 7) mfma<false, false, n == 0 && (i & 1) == 0>(ha[i >> 2].v[(i >> 1) & 1][nb], a, h2[4 * n + 2 * (i & 1) + nb]);
        else mfma<false, false, (i < 2)>(oc[n & 1].v[i & 1][nb], a, i < 8 ? own[2 * kb + nb] : par[2 * kb + nb]);
      };
      auto hconv = [&](auto U) { constexpr int k = 2 * (t - 102) + U;
        own[k >> 2][k & 3] = vrelucvt(EL(ha, k >> 2, 2 * (k & 3)), EL(ha, k >> 2, 2 * (k & 3) + 1)); };
      if constexpr ((t & 1) == 0) touch(af[(t + 1) % (PD + 1)]);
      mm(ic<0>{});
      if constexpr (t + PD < 224) {
        op_m0(ic<F_op(t)>{});
        if constexpr (rel != 0) alo = fread(ic<t + PD>{}, ic<0>{});
      }
      if constexpr (t >= 102 && t < 118) hconv(ic<0>{});
      if constexpr (t >= 118 && t < 122) par[2 * (t - 118) + 1] = rd128<(2 * (t - 118) + 1) * 1024>(bxr);
      if constexpr (t >= 130 && (t - 130) % 16 < 8) {
        constexpr int jp = (t - 130) / 16, k = (t - 130) % 16;
        ob[k >> 2][k & 3] = vcvt(EL(oc + ((jp + 1) & 1), k >> 2, 2 * (k & 3)), EL(oc + ((jp + 1) & 1), k >> 2, 2 * (k & 3) + 1));
      }
      mm(ic<1>{});
      if constexpr (t + PD < 224) {
        if constexpr (rel == 0) {
          bar<F_wait(wn)>();
          if constexpr (wn <= 6) landed(h2[4 * wn], h2[4 * wn + 1], h2[4 * wn + 2], h2[4 * wn + 3]);
          alo = fread(ic<t + PD>{}, ic<0>{});
        }
        af[(t + PD) % (PD + 1)] = __builtin_shufflevector(alo, fread(ic<t + PD>{}, ic<1>{}), 0, 1, 2, 3);
        op_go(ic<F_op(t)>{});
      }
      if constexpr (t >= 102 && t < 118) { hconv(ic<1>{}); if constexpr (t & 1) wr128<((t - 102) >> 1) * 1024>(bxw, own[(t - 102) >> 1]); }
      if constexpr (t == 117) xbar();
      if constexpr (t >= 117 && t < 121) par[2 * (t - 117)] = rd128<2 * (t - 117) * 1024>(bxr);
    }); });
    asm volatile("s_nop 7");
    sfor<8>([&](auto K) { ob[K >> 2][K & 3] = vcvt(EL(oc + 1, K >> 2, 2 * (K & 3)), EL(oc + 1, K >> 2, 2 * (K & 3) + 1)); });
    gstore<6 * 2048>(ob[0], vm, sG); gstore<6 * 2048 + 14336>(ob[1], vm, sG);
    asm volatile("s_waitcnt vmcnt(2)" : "+v"(fpv)::"memory");
  } else {
    op_go(ic<995>{});
    fpv = 0;
  }
  ld1(junk, (__builtin_amdgcn_ballot_w64(fpv != 1u) == 0 && (l & 31) < 24) ? vpf : 0x40000000u, mksrd(ws + WS_PF, 7 * FM_BYTES), 0);
  publish<1>(flags + 2048, g);

  await8(flags + 2048, w, l, g);
  {
    float a[16];
    sum_parts<7>(ws + WS_PF, fa, fb, a);
#pragma unroll
    for (int k = 0; k < 16; k++) a[k] = x1[k] + gb[k] * a[k];
    stu(out + row * 448 + ca, pack8(a));
    if (two) stu(out + row * 448 + cb_, pack8(a + 8));
  }
  asm volatile("" ::"v"(junk));
}

extern "C" __global__ __launch_bounds__(256) void k0_pre(const bf16_t* __restrict__ x, const bf16_t* __restrict__ c,
                                                         const bf16_t* __restrict__ wm, const bf16_t* __restrict__ wq,
                                                         const char* kc, const char* vc, const char* wo, const char* w1, const char* w2,
                                                         const char* out, char* ws) {
  __shared__ f4 red[4][8];
  const int t = threadIdx.x, l = t & 63, bx = blockIdx.x;
  if (bx < 84) {
    const bf16_t* wp = wm + (size_t)(t >> 2) * 2688 + bx * 32 + (t & 3) * 8;
    u4 wv[7]; float cv[7];
#pragma unroll
    for (int i = 0; i < 7; i++) cv[i] = bflo(c[(t >> 2) + 64 * i]);
#pragma unroll
    for (int i = 0; i < 7; i++) wv[i] = *(const u4*)(wp + (size_t)i * 64 * 2688);
    float acc[8] = {0.f, 0.f, 0.f, 0.f, 0.f, 0.f, 0.f, 0.f};
#pragma unroll
    for (int i = 0; i < 7; i++) {
      const float s = rbf(cv[i] * __builtin_amdgcn_rcpf(1.f + __expf(-cv[i])));
      float x8[8]; unpack8(wv[i], x8);
#pragma unroll
      for (int k = 0; k < 8; k++) acc[k] += s * x8[k];
    }
#pragma unroll
    for (int k = 0; k < 8; k++)
      acc[k] = grp_sum(acc[k] + __uint_as_float(__builtin_amdgcn_update_dpp(0u, __float_as_uint(acc[k]), 0x124, 0xf, 0xf, false)));
    if (l < 4) { red[t >> 6][2 * l] = f4{acc[0], acc[1], acc[2], acc[3]}; red[t >> 6][2 * l + 1] = f4{acc[4], acc[5], acc[6], acc[7]}; }
    __syncthreads();
    if (t < 32) {
      const float* r = (const float*)red;
      float s = rbf((r[t] + r[32 + t]) + (r[64 + t] + r[96 + t]));
      if ((bx >= 14 && bx < 28) || (bx >= 56 && bx < 70)) s = rbf(1.f + s);
      ((float*)(ws + WS_MOD))[bx * 32 + t] = s;
    }
  } else if (bx >= 152) {
    const int row = (bx - 152) * 32 + (t >> 6) * 8 + (l & 7), u = l >> 3;
    u4 xv[7]; float v[56];
#pragma unroll
    for (int i = 0; i < 7; i++) xv[i] = *(const u4*)(x + row * 448 + (u + 8 * i) * 8);
    float s = 0.f;
#pragma unroll
    for (int i = 0; i < 7; i++) unpack8(xv[i], v + 8 * i);
#pragma unroll
    for (int k = 0; k < 56; k++) s += v[k];
    const float mean = grp_sum(s) * (1.f / 448.f);
    s = 0.f;
#pragma unroll
    for (int k = 0; k < 56; k++) { v[k] -= mean; s += v[k] * v[k]; }
    const float rs = rsqrtf(grp_sum(s) * (1.f / 448.f) + 1e-5f);
#pragma unroll
    for (int k = 0; k < 56; k++) v[k] *= rs;
    char* np = ws + WS_N + (row >> 4) * 14336 + ((u & 4) | ((u & 1) << 1) | ((u >> 1) & 1)) * 256 + (row & 15) * 16;
#pragma unroll
    for (int i = 0; i < 7; i++) {
      const u4 p = pack8(v + 8 * i);
      const auto r0 = __builtin_amdgcn_permlane16_swap(p[0], p[2], false, false), r1 = __builtin_amdgcn_permlane16_swap(p[1], p[3], false, false);
      *(u4*)(np + i * 2048) = u4{r0[0], r1[0], r0[1], r1[1]};
    }
  } else if (bx >= 88) {
    const int b = (bx - 88) >> 3;
    const char* p;
    asm volatile("s_getpc_b64 s[40:41]\n\ts_add_u32 s40, s40, k1_attn@rel32@lo+4\n\ts_addc_u32 s41, s41, k1_attn@rel32@hi+12\n\ts_mov_b64 %0, s[40:41]"
                 : "=s"(p)::"scc", "s40", "s41");
    const int l0 = b * 256 + t, l1 = 2048 + b * 192 + t, l2 = 512 + b * 8 + (l & 7);
    const char* p0 = (const char*)(wq + (l0 >> 3) * 4096 + (bx & 7) * 512 + (l0 & 7) * 64);
    const char* p1 = t < 192 ? (const char*)(wq + (l1 >> 3) * 4096 + (bx & 7) * 512 + (l1 & 7) * 64) : p + (b * 64 + l) * 128;
    const char* p3 = l < 34 ? ws + WS_N + (size_t)(l >> 1) * FM_BYTES + ((l & 1) ? FM_BYTES - 128 : 64 * FM_TILE)
                   : l < 40 ? out + (l - 34) * 366976
                   : l < 48 ? ((l & 4) ? vc : kc) + ((bx & 7) * 2048 + 1536) * 512 + (l & 3) * 65536
                   : l < 56 ? wo + (bx & 7) * 458752 + (l & 7) * 57344 : ((l & 4) ? w2 : w1) + (l & 3) * 535168;
    unsigned j0, j1, j2, j3;
    if (t < 64) asm volatile("global_load_dword %0, %1, off" : "=v"(j3) : "v"(p3) : "memory");
    asm volatile("global_load_dword %0, %3, off\n\tglobal_load_dword %1, %4, off\n\tglobal_load_dword %2, %5, off\n\ts_waitcnt vmcnt(0)"
                 : "=&v"(j0), "=&v"(j1), "=&v"(j2) : "v"(p1), "v"(p0), "v"(p + l2 * 128) : "memory");
    asm volatile("" ::"v"(j3));
  } else {
    *(u4*)(ws + WS_FLAG + ((bx - 84) * 320 + t) * 16) = u4{0u, 0u, 0u, 0u};
    if (t < 64) *(u4*)(ws + WS_FLAG + ((bx - 84) * 320 + 256 + t) * 16) = u4{0u, 0u, 0u, 0u};
  }
}

extern "C" void kernel_launch(void* const* d_in, const int* in_sizes, int n_in, void* d_out, int out_size,
                              void* d_ws, size_t ws_size, hipStream_t stream) {
  const bf16_t* x = (const bf16_t*)d_in[0];
  char* ws = (char*)d_ws;
  (void)hipFuncSetAttribute((const void*)k1_attn, hipFuncAttributeMaxDynamicSharedMemorySize, LDS_BYTES);
  k0_pre<<<216, 256, 0, stream>>>(x, (const bf16_t*)d_in[8], (const bf16_t*)d_in[9], (const bf16_t*)d_in[3], (const char*)d_in[1],
                                  (const char*)d_in[2], (const char*)d_in[4], (const char*)d_in[6], (const char*)d_in[7], (const char*)d_out, ws);
  k1_attn<<<256, 256, LDS_BYTES, stream>>>((const bf16_t*)d_in[3], (const bf16_t*)d_in[1], (const bf16_t*)d_in[2], (const bf16_t*)d_in[4],
                                           (const bf16_t*)d_in[6], (const bf16_t*)d_in[7], x, (bf16_t*)d_out, ws);
}
```
